```python
import torch

def _softmax(x, axis=-1):
    return torch.softmax(x, dim=axis)
def _gelu(x):
    return torch.nn.functional.gelu(x, approximate="tanh")
def _silu(x):
    return torch.nn.functional.silu(x)
def _sigmoid(x):
    return torch.sigmoid(x)
def _layernorm(x, axis=-1, eps=1e-5):
    m = x.mean(dim=axis, keepdim=True)
    v = x.var(dim=axis, keepdim=True, unbiased=False)
    return (x - m) / torch.sqrt(v + eps)
def _scatter_add(tgt, idx, src, axis=0):
    return tgt.clone().index_add_(axis, idx, src)
def _segment_sum(src, seg, n):
    out = torch.zeros((n,) + src.shape[1:], dtype=src.dtype)
    return out.index_add_(0, seg, src)
def _pad(x, pw):
    flat = tuple(v for pair in reversed(pw) for v in pair)
    return torch.nn.functional.pad(x, flat)

kernel_name = 'gated_mix__linear_attn_block__dense_activation_gelu__prompt'

def setup_inputs():

    _w = (lambda a: (lambda t: t.to(torch.bfloat16) if t.is_floating_point() else t)(torch.as_tensor(a)))
    def _A_setup():
        B, S, D = 1, 2048, 512
        return {
            "x": _w((lambda *s: torch.randn(*s, dtype=torch.bfloat16) * 0.05)(B, S, D)),
            "wqkv": _w((lambda *s: torch.randn(*s, dtype=torch.bfloat16) * 0.8)(D, 3 * D)),
            "wo": _w((lambda *s: torch.randn(*s, dtype=torch.bfloat16) * 0.05)(D, D)),
        }
    _A_in = _A_setup()

    def _B_setup():
        B, S, D, F = 1, 2048, 512, 1376
        return {
            "x": _w((lambda *s: torch.randn(*s, dtype=torch.bfloat16) * 0.05)(B, S, D)),
            "w1": _w((lambda *s: torch.randn(*s, dtype=torch.bfloat16) * 3.2)(D, F) * (D ** -0.5)),
            "b1": _w((lambda *s: torch.randn(*s, dtype=torch.bfloat16) * 3.2)(F) * 0.01),
            "w2": _w((lambda *s: torch.randn(*s, dtype=torch.bfloat16) * 0.4)(F, D) * (F ** -0.5)),
            "b2": _w((lambda *s: torch.randn(*s, dtype=torch.bfloat16) * 0.8)(D) * 0.01),
        }
    _B_in = _B_setup()

    return {
        "A_x": _A_in["x"],
        "A_wqkv": _A_in["wqkv"],
        "A_wo": _A_in["wo"],
        "B_x": _B_in["x"],
        "B_w1": _B_in["w1"],
        "B_b1": _B_in["b1"],
        "B_w2": _B_in["w2"],
        "B_b2": _B_in["b2"],
        "gate_w": _w((lambda *s: torch.randn(*s, dtype=torch.bfloat16) * 0.05)(512) * 16.0),
    }

def reference(A_x, A_wqkv, A_wo, B_x, B_w1, B_b1, B_w2, B_b2, gate_w):
    def _A_ref(x, wqkv, wo):
        B, S, D = x.shape
        H = 32; Dh = D // H
        qkv = (lambda x, *d: x.permute(*d))((x @ wqkv).reshape(B, S, 3, H, Dh), 2, 0, 3, 1, 4)
        q, k, v = qkv[0], qkv[1], qkv[2]
        phi = lambda t: torch.where(t > 0, t + 1, torch.exp(t)) + 1e-6
        pq, pk = phi(q), phi(k)
        kv = torch.einsum("bhsd,bhse->bhde", pk, v)
        z = torch.einsum("bhsd->bhd", pk)
        num = torch.einsum("bhqd,bhde->bhqe", pq, kv)
        den = torch.einsum("bhqd,bhd->bhq", pq, z)[..., None] + 1e-6
        o = num / den
        return (lambda x, *d: x.permute(*d))(o, 0, 2, 1, 3).reshape(B, S, D) @ wo

    def _B_ref(x, w1, b1, w2, b2):
        h = torch.einsum("bsd,df->bsf", x, w1) + b1
        h = _gelu(h)
        return torch.einsum("bsf,fd->bsd", h, w2) + b2

    x = A_x
    g = _sigmoid((x * gate_w).sum(-1, keepdims=True))
    a = _A_ref(x, A_wqkv, A_wo)
    b = _B_ref(x, B_w1, B_b1, B_w2, B_b2)
    return g * a + (1 - g) * b
```

```cpp
#include <hip/hip_runtime.h>
#include <hip/hip_bf16.h>
typedef unsigned short u16;
__device__ __forceinline__ float bf2f(u16 v){ return __uint_as_float(((unsigned)v)<<16); }
__device__ __forceinline__ u16 f2bf(float f){ unsigned u=__float_as_uint(f); u += 0x7fffu + ((u>>16)&1u); return (u16)(u>>16); }
constexpr int S=2048,D=512,F=1376,H=32,DH=16;
__global__ void gemm_naive(const u16* A,const u16* B,float* C,int M,int N,int K){
  int n=blockIdx.x*64+threadIdx.x%64; int m=blockIdx.y*4+threadIdx.x/64;
  if(n>=N||m>=M)return; float s=0; for(int k=0;k<K;++k) s+=bf2f(A[m*K+k])*bf2f(B[k*N+n]); C[m*N+n]=s; }
__device__ __forceinline__ float phi(float t){ return (t>0? t+1.f: __expf(t))+1e-6f; }
__global__ void kv_k(const float* qkv,float* kv,float* z){
  int h=blockIdx.x; int d=threadIdx.x/16,e=threadIdx.x%16; float s=0,zz=0;
  for(int i=0;i<S;++i){ float pk=phi(qkv[i*1536+512+h*16+d]); s+=pk*qkv[i*1536+1024+h*16+e]; zz+=pk; }
  kv[h*256+d*16+e]=s; if(e==0) z[h*16+d]=zz; }
__global__ void o_k(const float* qkv,const float* kv,const float* z,u16* o){
  int i=blockIdx.x; int c=threadIdx.x; int h=c/16,e=c%16; float num=0,den=0;
  for(int d=0;d<16;++d){ float pq=phi(qkv[i*1536+h*16+d]); num+=pq*kv[h*256+d*16+e]; den+=pq*z[h*16+d]; }
  o[i*512+c]=f2bf(num/(den+1e-6f)); }
__global__ void gelu_k(const float* hp,const u16* b1,u16* h){ int i=blockIdx.x; for(int c=threadIdx.x;c<F;c+=256){ float v=hp[i*F+c]+bf2f(b1[c]); float t=tanhf(0.7978845608028654f*(v+0.044715f*v*v*v)); h[i*F+c]=f2bf(0.5f*v*(1.f+t)); } }
__global__ void fin_k(const u16* x,const u16* gw,const float* a,const float* b,const u16* b2,u16* out){
  int i=blockIdx.x; __shared__ float red[512]; int c=threadIdx.x; red[c]=bf2f(x[i*512+c])*bf2f(gw[c]); __syncthreads();
  for(int s=256;s>0;s>>=1){ if(c<s) red[c]+=red[c+s]; __syncthreads(); }
  float g=1.f/(1.f+__expf(-red[0])); out[i*512+c]=f2bf(g*a[i*512+c]+(1.f-g)*(b[i*512+c]+bf2f(b2[c]))); }
extern "C" void kernel_launch(void* const* d_in, const int* in_sizes, int n_in, void* d_out, int out_size, void* d_ws, size_t ws_size, hipStream_t stream) {
  const u16* x=(const u16*)d_in[0]; const u16* wqkv=(const u16*)d_in[1]; const u16* wo=(const u16*)d_in[2];
  const u16* w1=(const u16*)d_in[4]; const u16* b1=(const u16*)d_in[5]; const u16* w2=(const u16*)d_in[6]; const u16* b2=(const u16*)d_in[7]; const u16* gw=(const u16*)d_in[8];
  float* qkv=(float*)d_ws; float* hp=qkv+S*1536; float* a=hp+S*F; float* b=a+S*D; float* kv=b+S*D; float* z=kv+H*256; u16* o=(u16*)(z+H*16); u16* h=o+S*D;
  gemm_naive<<<dim3(1536/64,S/4),256,0,stream>>>(x,wqkv,qkv,S,1536,D);
  gemm_naive<<<dim3((F+63)/64,S/4),256,0,stream>>>(x,w1,hp,S,F,D);
  kv_k<<<H,256,0,stream>>>(qkv,kv,z);
  o_k<<<S,512,0,stream>>>(qkv,kv,z,o);
  gelu_k<<<S,256,0,stream>>>(hp,b1,h);
  gemm_naive<<<dim3(D/64,S/4),256,0,stream>>>(o,wo,a,S,D,D);
  gemm_naive<<<dim3(D/64,S/4),256,0,stream>>>(h,w2,b,S,D,F);
  fin_k<<<S,512,0,stream>>>(x,gw,a,b,b2,(u16*)d_out);
}
```

```cpp
#include <hip/hip_runtime.h>
#include <hip/hip_bf16.h>

typedef unsigned long long u64;
typedef unsigned int u32;
typedef short s16x4 __attribute__((ext_vector_type(4)));
typedef __bf16 bf8 __attribute__((ext_vector_type(8)));
typedef __bf16 bf2 __attribute__((ext_vector_type(2)));
typedef float f2 __attribute__((ext_vector_type(2)));
typedef float f4 __attribute__((ext_vector_type(4)));
typedef float f16v __attribute__((ext_vector_type(16)));
typedef u32 u2 __attribute__((ext_vector_type(2)));
typedef u32 u4 __attribute__((ext_vector_type(4)));
typedef __amdgpu_buffer_rsrc_t rsrc_t;
#define LAS __attribute__((address_space(3)))
#define DI __device__ __forceinline__

constexpr int S_ = 2048, D_ = 512, F_ = 1376;
constexpr u32 NSTEP = 118;
constexpr u32 WS_EPOCH = 0;
constexpr u32 WS_FLAG = 4096;
constexpr u32 WS_KVP = WS_FLAG + 16 * 16 * 8 * 2 * 64;
constexpr u32 WS_ZP = WS_KVP + 256 * 2048;
constexpr u32 WS_G = WS_ZP + 256 * 128;
constexpr u32 WS_A2 = WS_G + 2048 * 4;
constexpr u32 WS_END = WS_A2 + 64 * NSTEP * 1024;
constexpr u32 WS_DBG = WS_END;
constexpr u32 LDS_BYTES = 163840;
constexpr u32 SLOT = 32768, MISC = 2 * SLOT + 16384;
constexpr u32 OOB = 0x7ffffff0u;
constexpr u32 SPIN_CAP = 1u << 18;

DI rsrc_t mk_rsrc(const void* p, u32 bytes) { return __builtin_amdgcn_make_buffer_rsrc((void*)p, (short)0, (int)bytes, 0x00020000); }
DI void dma(u32 lds, u32 voff, rsrc_t r, u32 soff) {
  asm volatile("s_mov_b32 m0, %0\n\ts_nop 0\n\tbuffer_load_dwordx4 %1, %2, %3 offen lds" ::"s"(lds), "v"(voff), "s"(r), "s"(soff) : "memory");
}
DI u4 ld16_sc1(u32 voff, rsrc_t r, u32 soff) { u4 v; asm volatile("buffer_load_dwordx4 %0, %1, %2, %3 offen sc1" : "=v"(v) : "v"(voff), "s"(r), "s"(soff) : "memory"); return v; }
DI u2 ld8_sc1(u32 voff, rsrc_t r, u32 soff) { u2 v; asm volatile("buffer_load_dwordx2 %0, %1, %2, %3 offen sc1" : "=v"(v) : "v"(voff), "s"(r), "s"(soff) : "memory"); return v; }
DI u32 ld4_sc1(u32 voff, rsrc_t r, u32 soff) { u32 v; asm volatile("buffer_load_dword %0, %1, %2, %3 offen sc1" : "=v"(v) : "v"(voff), "s"(r), "s"(soff) : "memory"); return v; }
DI u2 ld8(u32 voff, rsrc_t r, u32 soff) { u2 v; asm volatile("buffer_load_dwordx2 %0, %1, %2, %3 offen" : "=v"(v) : "v"(voff), "s"(r), "s"(soff) : "memory"); return v; }
DI void st16_sc1(u4 v, u32 voff, rsrc_t r, u32 soff) { asm volatile("buffer_store_dwordx4 %0, %1, %2, %3 offen sc1\n\ts_nop 1" ::"v"(v), "v"(voff), "s"(r), "s"(soff) : "memory"); }
DI void st8_sc1(u2 v, u32 voff, rsrc_t r, u32 soff) { asm volatile("buffer_store_dwordx2 %0, %1, %2, %3 offen sc1\n\ts_nop 1" ::"v"(v), "v"(voff), "s"(r), "s"(soff) : "memory"); }
DI void st4_sc1(u32 v, u32 voff, rsrc_t r, u32 soff) { asm volatile("buffer_store_dword %0, %1, %2, %3 offen sc1\n\ts_nop 1" ::"v"(v), "v"(voff), "s"(r), "s"(soff) : "memory"); }
#define WAITV(n) asm volatile("s_waitcnt vmcnt(" #n ")" ::: "memory")
#define BARV(n) asm volatile("s_waitcnt vmcnt(" #n ") lgkmcnt(0)\n\ts_barrier" ::: "memory")
#define BAR() asm volatile("s_waitcnt lgkmcnt(0)\n\ts_barrier" ::: "memory")
#define PIN(x) asm volatile("" : "+v"(x))
#define SCHED() __builtin_amdgcn_sched_barrier(0)
template <int N> DI void waitv() { asm volatile("s_waitcnt vmcnt(%0)" ::"i"(N) : "memory"); }

DI bf8 lds_b128(u32 a) { return *(LAS bf8*)(size_t)a; }
DI bf8 lds_tr(u32 a, u32 d) {
  s16x4 lo = __builtin_amdgcn_ds_read_tr16_b64_v4i16((LAS s16x4*)(size_t)a);
  s16x4 hi = __builtin_amdgcn_ds_read_tr16_b64_v4i16((LAS s16x4*)(size_t)(a + d));
  return __builtin_bit_cast(bf8, __builtin_shufflevector(lo, hi, 0, 1, 2, 3, 4, 5, 6, 7));
}
DI f16v mfma(bf8 a, bf8 b, f16v c) { return __builtin_amdgcn_mfma_f32_32x32x16_bf16(a, b, c, 0, 0, 0); }
DI u32 pk2(float a, float b) { f2 v = {a, b}; return __builtin_bit_cast(u32, __builtin_convertvector(v, bf2)); }
DI u4 pack8(const f16v& x, int s) { u4 r = {pk2(x[8 * s], x[8 * s + 1]), pk2(x[8 * s + 2], x[8 * s + 3]), pk2(x[8 * s + 4], x[8 * s + 5]), pk2(x[8 * s + 6], x[8 * s + 7])}; return r; }
DI float bflo(u32 v) { return __uint_as_float(v << 16); }
DI float bfhi(u32 v) { return __uint_as_float(v & 0xffff0000u); }
DI float phi(float t) { float e = __builtin_amdgcn_exp2f(t * 1.4426950408889634f); return (t > 0.f ? t + 1.f : e) + 1e-6f; }
DI float gelu(float v) { float e = __builtin_amdgcn_exp2f(v * (-2.3022081985f - 0.1029432397f * v * v)); return v * __builtin_amdgcn_rcpf(1.f + e); }
DI void bias16(f16v& a, const u2* b) {
#pragma unroll
  for (int g = 0; g < 4; ++g) { a[4 * g] = bflo(b[g].x); a[4 * g + 1] = bfhi(b[g].x); a[4 * g + 2] = bflo(b[g].y); a[4 * g + 3] = bfhi(b[g].y); }
}
DI u32 flag_off(u32 mt, u32 ng, u32 wv, u32 kind) { return WS_FLAG + ((((mt * 16 + ng) * 8 + wv) * 2) + kind) * 64; }
DI void poll(u32 voff, rsrc_t ws, u32 tlo, u32 thi) {
#pragma nounroll
  for (u32 n = 0; n < SPIN_CAP; ++n) {
    u2 v = ld8_sc1(voff, ws, 0);
    asm volatile("s_waitcnt vmcnt(0)" : "+v"(v)::"memory");
    bool ok = (voff == OOB) || (v.x == tlo && v.y == thi);
    if (__all(ok)) break;
    __builtin_amdgcn_s_sleep(1);
  }
}

#define ST(i)

struct Ctx {
  rsrc_t rx, rw, rw2, rwo, rws;
  u32 w, lane, grp, tg, hh;
  u32 mt, ng, s0, stride;
  u32 vx0, vx1, vw0, vw1, vp2;
  u32 vb2, th, ks4, nt, tg32;
  u32 tlo, thi;
};
DI u32 step_id(const Ctx& c, int e) { return e < 22 ? 22 * c.ks4 + e : 86 + 8 * c.ks4 + (e - 22); }
DI u32 b2_lds(const Ctx& c, int e) {
  return e < 4 ? c.ks4 * 8192 + e * 2048 : e < 8 ? SLOT + c.ks4 * 8192 + (e - 4) * 2048 : e < 10 ? 2 * SLOT + c.ks4 * 4096 + (e - 8) * 2048
       : e < 20 ? MISC + c.ks4 * 20480 + (e - 10) * 2048 : c.ks4 * 20480 + (e - 20) * 2048;
}
DI void b2_dma(const Ctx& c, int e) {
  if (e < 22) dma(b2_lds(c, e) + c.th * 1024, c.vb2, c.rw2, (22 * c.ks4 + e) * 16384);
  else dma(b2_lds(c, e) + c.th * 1024, c.vb2, c.rwo, (8 * c.ks4 + (e - 22)) * 16384);
}
template <int ST_> DI void issue_stage(const Ctx& c) {
  constexpr u32 base = (ST_ % 5) * SLOT;
  if constexpr (ST_ < 8) {
    const u32 kb = (c.s0 + ST_) & 7;
    dma(base + c.w * 2048, c.vx0, c.rx, kb * 128);
    dma(base + c.w * 2048 + 1024, c.vx1, c.rx, kb * 128);
    dma(base + 16384 + c.w * 2048, c.vw0, c.rw, kb * 64 * c.stride);
    dma(base + 16384 + c.w * 2048 + 1024, c.vw1, c.rw, kb * 64 * c.stride);
  } else if constexpr (ST_ < 10) {
#pragma unroll
    for (int n = 0; n < 4; ++n) {
      const u32 kb = (c.s0 + (ST_ - 8) * 4 + n) & 7;
      dma(base + (c.w + 8 * n) * 1024, c.vp2, c.rw, kb * 64 * c.stride);
    }
  } else if constexpr (ST_ < 12) {
#pragma unroll
    for (int n = 0; n < 4; ++n) b2_dma(c, (ST_ - 10) * 4 + n);
  } else {
    b2_dma(c, 8); b2_dma(c, 9);
  }
}

template <bool QKV, int s> DI void p1_stage(const Ctx& c, bf8 (&xf)[32], bf8 (&w0)[4], bf8 (&w1)[4], f16v& a0, f16v& a1,
                                            const u32 (&ax)[4], u32 aw0, u32 aw1) {
  constexpr u32 base = (s % 5) * SLOT, nb = ((s + 1) % 5) * SLOT;
  xf[4 * s + 2] = lds_b128(ax[2] + base); xf[4 * s + 3] = lds_b128(ax[3] + base);
  w1[0] = lds_tr(aw0 + base + 2 * 4096, 1024); w1[1] = lds_tr(aw1 + base + 2 * 4096, 1024);
  w1[2] = lds_tr(aw0 + base + 3 * 4096, 1024); w1[3] = lds_tr(aw1 + base + 3 * 4096, 1024);
  if constexpr (QKV) { a0 = mfma(xf[4 * s], w0[0], a0); a1 = mfma(xf[4 * s], w0[1], a1); a0 = mfma(xf[4 * s + 1], w0[2], a0); a1 = mfma(xf[4 * s + 1], w0[3], a1); }
  else { a0 = mfma(w0[0], xf[4 * s], a0); a1 = mfma(w0[1], xf[4 * s], a1); a0 = mfma(w0[2], xf[4 * s + 1], a0); a1 = mfma(w0[3], xf[4 * s + 1], a1); }
  SCHED();
  asm volatile("s_waitcnt vmcnt(12) lgkmcnt(0)\n\ts_barrier" : "+v"(a0), "+v"(a1)::"memory");
  issue_stage<s + 5>(c);
  SCHED();
  if constexpr (s < 7) {
    xf[4 * s + 4] = lds_b128(ax[0] + nb); xf[4 * s + 5] = lds_b128(ax[1] + nb);
    w0[0] = lds_tr(aw0 + nb, 1024); w0[1] = lds_tr(aw1 + nb, 1024);
    w0[2] = lds_tr(aw0 + nb + 4096, 1024); w0[3] = lds_tr(aw1 + nb + 4096, 1024);
  }
  if constexpr (QKV) { a0 = mfma(xf[4 * s + 2], w1[0], a0); a1 = mfma(xf[4 * s + 2], w1[1], a1); a0 = mfma(xf[4 * s + 3], w1[2], a0); a1 = mfma(xf[4 * s + 3], w1[3], a1); }
  else { a0 = mfma(w1[0], xf[4 * s + 2], a0); a1 = mfma(w1[1], xf[4 * s + 2], a1); a0 = mfma(w1[2], xf[4 * s + 3], a0); a1 = mfma(w1[3], xf[4 * s + 3], a1); }
}
template <bool QKV> DI void pass1(const Ctx& c, bf8 (&xf)[32], f16v& a0, f16v& a1) {
  const u32 l = c.lane, r = l & 31, q = (l & 15) >> 2, p = l & 3, blk = (l >> 4) & 1;
  const u32 e = c.hh ^ ((r >> 1) & 7);
  u32 ax[4];
#pragma unroll
  for (int s = 0; s < 4; ++s) ax[s] = (c.tg * 32 + r) * 128 + (((2 * s) ^ e) * 16);
  const u32 awb = 16384 + (8 * c.hh + q) * 256 + blk * 32 + p * 8;
  const u32 aw0 = awb + ((c.grp ^ q) * 64), aw1 = awb + (((c.grp + 2) ^ q) * 64);
  bf8 w0[4], w1[4];
  xf[0] = lds_b128(ax[0]); xf[1] = lds_b128(ax[1]);
  w0[0] = lds_tr(aw0, 1024); w0[1] = lds_tr(aw1, 1024); w0[2] = lds_tr(aw0 + 4096, 1024); w0[3] = lds_tr(aw1 + 4096, 1024);
  p1_stage<QKV, 0>(c, xf, w0, w1, a0, a1, ax, aw0, aw1); p1_stage<QKV, 1>(c, xf, w0, w1, a0, a1, ax, aw0, aw1);
  p1_stage<QKV, 2>(c, xf, w0, w1, a0, a1, ax, aw0, aw1); p1_stage<QKV, 3>(c, xf, w0, w1, a0, a1, ax, aw0, aw1);
  p1_stage<QKV, 4>(c, xf, w0, w1, a0, a1, ax, aw0, aw1); p1_stage<QKV, 5>(c, xf, w0, w1, a0, a1, ax, aw0, aw1);
  p1_stage<QKV, 6>(c, xf, w0, w1, a0, a1, ax, aw0, aw1); p1_stage<QKV, 7>(c, xf, w0, w1, a0, a1, ax, aw0, aw1);
  asm volatile("" : "+v"(a0), "+v"(a1));
}
template <int U0> DI void pass2_half(const Ctx& c, const bf8 (&xf)[32], f16v& a) {
  const u32 l = c.lane, q = (l & 15) >> 2, p = l & 3, blk = (l >> 4) & 1;
  const u32 ab = 3 * SLOT + (8 * c.hh + q) * 128 + ((c.grp ^ (q >> 1)) * 64) + blk * 32 + p * 8;
#pragma unroll
  for (int u = U0; u < U0 + 16; ++u) a = mfma(lds_tr(ab + u * 2048, 512), xf[u], a);
}
DI void store_chunk(const Ctx& c, const f16v& a, u32 step0) {
  const u32 so = WS_A2 + ((c.mt * 4 + c.tg) * NSTEP + step0) * 1024;
  st16_sc1(pack8(a, 0), c.lane * 16, c.rws, so);
  st16_sc1(pack8(a, 1), c.lane * 16, c.rws, so + 1024);
}
DI void gelu_store(const Ctx& c, f16v& a, u32 chunk) {
#pragma unroll
  for (int i = 0; i < 16; ++i) a[i] = gelu(a[i]);
  store_chunk(c, a, 2 * chunk);
}
DI void put_flag(const Ctx& c, u32 kind) { u2 t = {c.tlo, c.thi}; st8_sc1(t, c.lane == 0 ? flag_off(c.mt, c.ng, c.w, kind) : OOB, c.rws, 0); }

__global__ __launch_bounds__(512, 2) void gm_fused(const void* __restrict__ x, const void* __restrict__ wqkv, const void* __restrict__ wo,
                                                   const void* __restrict__ w1, const void* __restrict__ b1, const void* __restrict__ w2,
                                                   const void* __restrict__ b2, const void* __restrict__ gw, void* __restrict__ out, void* __restrict__ ws) {
  ST(0);
  Ctx c;
  const u32 b = blockIdx.x, xcd = b & 7, bi = b >> 3;
  c.lane = threadIdx.x & 63; c.w = __builtin_amdgcn_readfirstlane(threadIdx.x >> 6);
  c.grp = c.w >> 2; c.tg = c.w & 3; c.hh = c.lane >> 5;
  const u32 l = c.lane, jj = bi >> 3;
  const bool isq = jj < 2;
  const u32 g8 = 2 * (xcd >> 1) + (jj & 1);
  c.mt = 8 * (xcd & 1) + (bi & 7); c.ng = isq ? g8 : 8 + g8; c.s0 = ((bi & 7) + 2 * jj) & 7;
  const u32 cb0 = (g8 >> 1) * 11 + ((g8 & 1) ? (g8 == 7 ? 5 : 6) : 0);
  const bool six = (g8 & 1) == 0 && g8 < 6;
  c.rx = mk_rsrc(x, S_ * D_ * 2); c.rws = mk_rsrc(ws, WS_END);
  c.rw = isq ? mk_rsrc(wqkv, D_ * 1536 * 2) : mk_rsrc(w1, D_ * F_ * 2);
  c.rw2 = mk_rsrc(w2, F_ * D_ * 2); c.rwo = mk_rsrc(wo, D_ * D_ * 2);
  c.stride = isq ? 3072 : 2752;
  const u32 base0 = isq ? (512 + 64 * g8) * 2 : cb0 * 64, delta = isq ? 1024 : 128, base2 = isq ? 128 * g8 : cb0 * 64 + 256;
  c.vx0 = (c.mt * 128 + 16 * c.w + (l >> 3)) * 1024 + (((l & 7) ^ (l >> 4)) * 16);
  c.vx1 = (c.mt * 128 + 16 * c.w + 8 + (l >> 3)) * 1024 + (((l & 7) ^ (4 + (l >> 4))) * 16);
  { const u32 qd = ((l >> 2) & 3) ^ (l >> 4);
    c.vw0 = (8 * c.w + (l >> 4)) * c.stride + base0 + (qd >> 1) * delta + (qd & 1) * 64 + (l & 3) * 16; c.vw1 = c.vw0 + 4 * c.stride; }
  const u32 hsw = (((l >> 2) & 1) ^ ((l >> 4) & 1)) * 64 + (l & 3) * 16;
  c.vp2 = (8 * c.w + (l >> 3)) * c.stride + base2 + hsw;
  c.th = c.w & 1; c.ks4 = c.w >> 1; c.nt = bi >> 2; c.tg32 = 2 * (4 * xcd + (bi & 3)) + c.th;
  c.vb2 = (8 * c.th + (l >> 3)) * 1024 + c.nt * 128 + hsw;

  asm volatile("s_nop 4" ::: "memory");
  u2 ep = ld8_sc1(0, c.rws, WS_EPOCH);
  u2 bb[3][4];
  { const rsrc_t rb = mk_rsrc(b1, F_ * 2);
#pragma unroll
    for (int k = 0; k < 3; ++k)
#pragma unroll
      for (int g = 0; g < 4; ++g) bb[k][g] = ld8(isq ? OOB : ((cb0 + c.grp + 2 * k) * 32 + 4 * c.hh) * 2 + g * 16, rb, 0); }
  issue_stage<0>(c); issue_stage<1>(c); issue_stage<2>(c); issue_stage<3>(c); issue_stage<4>(c);
  asm volatile("s_waitcnt vmcnt(16) lgkmcnt(0)\n\ts_barrier" : "+v"(ep), "+v"(bb[0][0]), "+v"(bb[0][1]), "+v"(bb[0][2]), "+v"(bb[0][3]), "+v"(bb[1][0]), "+v"(bb[1][1]),
               "+v"(bb[1][2]), "+v"(bb[1][3]), "+v"(bb[2][0]), "+v"(bb[2][1]), "+v"(bb[2][2]), "+v"(bb[2][3])::"memory");
  { const u64 E = ((u64)(u32)__builtin_amdgcn_readfirstlane(ep.y) << 32) | (u32)__builtin_amdgcn_readfirstlane(ep.x);
    const u64 T = E * 6364136223846793005ULL + 1442695040888963407ULL; c.tlo = (u32)T; c.thi = (u32)(T >> 32); }
  ST(1);

  {
    bf8 xf[32];
    if (isq) {
      f16v ak = {}, av = {};
      pass1<true>(c, xf, ak, av);
      ST(2);
      float zs = 0.f;
#pragma unroll
      for (int i = 0; i < 16; ++i) { ak[i] = phi(ak[i]); zs += ak[i]; }
      f16v kv = {};
      kv = mfma(__builtin_bit_cast(bf8, pack8(ak, 0)), __builtin_bit_cast(bf8, pack8(av, 0)), kv);
      kv = mfma(__builtin_bit_cast(bf8, pack8(ak, 1)), __builtin_bit_cast(bf8, pack8(av, 1)), kv);
      zs += __shfl_xor(zs, 32);
      const bool he = (l >> 4) & 1;
      f4 s0v, s1v;
#pragma unroll
      for (int i = 0; i < 4; ++i) { s0v[i] = he ? kv[8 + i] : kv[i]; s1v[i] = he ? kv[12 + i] : kv[4 + i]; }
      const u32 mb = MISC + c.grp * 6144, zb = MISC + 12288 + c.grp * 512;
      if (c.tg != 0) { *(LAS f4*)(size_t)(mb + (c.tg - 1) * 2048 + l * 16) = s0v; *(LAS f4*)(size_t)(mb + (c.tg - 1) * 2048 + 1024 + l * 16) = s1v; }
      if (l < 32) *(LAS float*)(size_t)(zb + c.tg * 128 + l * 4) = zs;
      BARV(10);
      if (c.tg == 0) {
#pragma unroll
        for (int t = 0; t < 3; ++t) { s0v += *(LAS f4*)(size_t)(mb + t * 2048 + l * 16); s1v += *(LAS f4*)(size_t)(mb + t * 2048 + 1024 + l * 16); }
        const u32 zl = zb + (l & 31) * 4;
        const float zt = (*(LAS float*)(size_t)(zl) + *(LAS float*)(size_t)(zl + 128)) + (*(LAS float*)(size_t)(zl + 256) + *(LAS float*)(size_t)(zl + 384));
        const u32 pi = (c.mt * 8 + g8) * 2 + c.grp;
        st16_sc1(__builtin_bit_cast(u4, s0v), l * 16, c.rws, WS_KVP + pi * 2048);
        st16_sc1(__builtin_bit_cast(u4, s1v), l * 16, c.rws, WS_KVP + pi * 2048 + 1024);
        st4_sc1(__float_as_uint(zt), l < 32 ? l * 4 : OOB, c.rws, WS_ZP + pi * 128);
        WAITV(0);
        put_flag(c, 1);
      }
      ST(3);
      f16v aq = {};
      pass2_half<0>(c, xf, aq); pass2_half<16>(c, xf, aq);
#pragma unroll
      for (int i = 0; i < 16; ++i) aq[i] = phi(aq[i]);
      const u4 pq0 = pack8(aq, 0), pq1 = pack8(aq, 1);
      ST(4);
      poll(l < 4 ? flag_off(4 * c.tg + l, g8, 4 * c.grp, 1) : OOB, c.rws, c.tlo, c.thi);
      ST(5);
      u4 pa[4], pb[4]; u32 pz[4];
#pragma unroll
      for (int m = 0; m < 4; ++m) {
        const u32 pi = ((4 * c.tg + m) * 8 + g8) * 2 + c.grp;
        pa[m] = ld16_sc1(l * 16, c.rws, WS_KVP + pi * 2048); pb[m] = ld16_sc1(l * 16, c.rws, WS_KVP + pi * 2048 + 1024);
        pz[m] = ld4_sc1((l & 31) * 4, c.rws, WS_ZP + pi * 128);
      }
      asm volatile("s_waitcnt vmcnt(0) lgkmcnt(0)\n\ts_barrier" : "+v"(pa[0]), "+v"(pa[1]), "+v"(pa[2]), "+v"(pa[3]), "+v"(pb[0]), "+v"(pb[1]), "+v"(pb[2]), "+v"(pb[3]),
                   "+v"(pz[0]), "+v"(pz[1]), "+v"(pz[2]), "+v"(pz[3])::"memory");
      const f4 ka = (__builtin_bit_cast(f4, pa[0]) + __builtin_bit_cast(f4, pa[1])) + (__builtin_bit_cast(f4, pa[2]) + __builtin_bit_cast(f4, pa[3]));
      const f4 kb = (__builtin_bit_cast(f4, pb[0]) + __builtin_bit_cast(f4, pb[1])) + (__builtin_bit_cast(f4, pb[2]) + __builtin_bit_cast(f4, pb[3]));
      const float kz = (__uint_as_float(pz[0]) + __uint_as_float(pz[1])) + (__uint_as_float(pz[2]) + __uint_as_float(pz[3]));
      const u32 rb = MISC + c.grp * 8192, rz = 3 * SLOT + c.grp * 512;
      *(LAS f4*)(size_t)(rb + c.tg * 2048 + l * 16) = ka; *(LAS f4*)(size_t)(rb + c.tg * 2048 + 1024 + l * 16) = kb;
      if (l < 32) *(LAS float*)(size_t)(rz + c.tg * 128 + l * 4) = kz;
      BAR();
      f4 fa = {}, fb = {}, za = {}, zc = {};
      const u32 zo = rz + (((l >> 4) & 1) * 16 + 4 * c.hh) * 4;
#pragma unroll
      for (int t = 0; t < 4; ++t) {
        fa += *(LAS f4*)(size_t)(rb + t * 2048 + l * 16); fb += *(LAS f4*)(size_t)(rb + t * 2048 + 1024 + l * 16);
        za += *(LAS f4*)(size_t)(zo + t * 128); zc += *(LAS f4*)(size_t)(zo + t * 128 + 32);
      }
      const u4 kvp = {pk2(fa[0], fa[1]), pk2(fa[2], fa[3]), pk2(fb[0], fb[1]), pk2(fb[2], fb[3])};
      const u4 zp = {pk2(za[0], za[1]), pk2(za[2], za[3]), pk2(zc[0], zc[1]), pk2(zc[2], zc[3])};
      const u4 zero = {0, 0, 0, 0};
      f16v num = {}, den = {};
      num = mfma(__builtin_bit_cast(bf8, he ? zero : kvp), __builtin_bit_cast(bf8, pq0), num);
      num = mfma(__builtin_bit_cast(bf8, he ? kvp : zero), __builtin_bit_cast(bf8, pq1), num);
      den = mfma(__builtin_bit_cast(bf8, he ? zero : zp), __builtin_bit_cast(bf8, pq0), den);
      den = mfma(__builtin_bit_cast(bf8, he ? zp : zero), __builtin_bit_cast(bf8, pq1), den);
      const float r0 = __builtin_amdgcn_rcpf(den[0] + 1e-6f), r1 = __builtin_amdgcn_rcpf(den[8] + 1e-6f);
#pragma unroll
      for (int i = 0; i < 8; ++i) { num[i] *= r0; num[8 + i] *= r1; }
      store_chunk(c, num, 86 + 4 * g8 + 2 * c.grp);
    } else {
      f16v a0, a1, a2;
      bias16(a0, bb[0]); bias16(a1, bb[1]);
      pass1<false>(c, xf, a0, a1);
      ST(2);
      bias16(a2, bb[2]);
      if (c.grp == 0) {
        pass2_half<0>(c, xf, a2);
        BARV(10);
        pass2_half<16>(c, xf, a2);
        ST(3);
        gelu_store(c, a0, cb0); gelu_store(c, a1, cb0 + 2); gelu_store(c, a2, cb0 + 4);
      } else {
        const rsrc_t rg = mk_rsrc(gw, D_ * 2);
        dma(MISC + c.tg * 1024, l * 16, rg, 0);
        gelu_store(c, a0, cb0 + 1);
        BARV(13);
        gelu_store(c, a1, cb0 + 3);
        ST(3);
        if (six) {
          pass2_half<0>(c, xf, a2); pass2_half<16>(c, xf, a2);
          gelu_store(c, a2, cb0 + 5);
        } else if (g8 == 7) {
          WAITV(4);
          f16v ag = {};
#pragma unroll
          for (int u = 0; u < 32; ++u) ag = mfma(lds_b128(MISC + c.tg * 1024 + ((((c.s0 + (u >> 2)) & 7) * 64 + 16 * (u & 3) + 8 * c.hh) * 2)), xf[u], ag);
          const float g = __builtin_amdgcn_rcpf(1.f + __builtin_amdgcn_exp2f(-1.4426950408889634f * ag[0]));
          st4_sc1(__float_as_uint(g), l < 32 ? l * 4 : OOB, c.rws, WS_G + (c.mt * 128 + c.tg * 32) * 4);
        }
      }
    }
  }
  ST(6);
  BARV(0);
  put_flag(c, 0);
  ST(7);
#pragma unroll
  for (int e = 10; e < 20; ++e) b2_dma(c, e);
  const u32 q = (l & 15) >> 2, tb = (4 * c.hh + q) * 128 + ((l >> 4) & 1) * 32 + (l & 3) * 8;
  const u32 tb0 = tb + ((q >> 1) * 64), tb1 = tb + (((q >> 1) ^ 1) * 64);
  bf8 bw[10][2];
#pragma unroll
  for (int e = 0; e < 10; ++e) { bw[e][0] = lds_tr(tb0 + b2_lds(c, e), 1024); bw[e][1] = lds_tr(tb1 + b2_lds(c, e), 1024); }
  BAR();
#pragma unroll
  for (int e = 20; e < 30; ++e) b2_dma(c, e);
  f16v m0 = {}, m1 = {};
  u2 b2v[2][4];
  { const rsrc_t rb = mk_rsrc(b2, D_ * 2);
#pragma unroll
    for (int k = 0; k < 2; ++k)
#pragma unroll
      for (int g = 0; g < 4; ++g) b2v[k][g] = ld8(c.ks4 == 0 ? (c.nt * 64 + k * 32 + 4 * c.hh) * 2 + g * 16 : OOB, rb, 0); }
  const u32 pm = c.tg32 >> 2, pt = c.tg32 & 3;
  ST(8);
  poll(l < 4 ? flag_off(pm, 8 + 2 * c.ks4 + (l >> 1), (l & 1) * 4 + pt, 0) : l == 4 ? flag_off(pm, 15, 4 + pt, 0) : OOB, c.rws, c.tlo, c.thi);
  asm volatile("" : "+v"(b2v[0][0]), "+v"(b2v[0][1]), "+v"(b2v[0][2]), "+v"(b2v[0][3]), "+v"(b2v[1][0]), "+v"(b2v[1][1]), "+v"(b2v[1][2]), "+v"(b2v[1][3]));
  ST(9);
  bias16(m0, b2v[0]); bias16(m1, b2v[1]);
  u4 af[22];
  const u32 abase = WS_A2 + c.tg32 * NSTEP * 1024;
#pragma unroll
  for (int e = 0; e < 22; ++e) { u32 id = 22 * c.ks4 + e; if (e >= 20) id = id > 85 ? 85 : id; af[e] = ld16_sc1(l * 16, c.rws, abase + id * 1024); }
  u32 gv = ld4_sc1((l & 31) * 4, c.rws, WS_G + c.tg32 * 128);
  BAR();
#define P2STEP(e, n) do { asm volatile("s_waitcnt vmcnt(" #n ")" : "+v"(af[e])::"memory"); \
    const bf8 wa = e < 10 ? bw[e < 10 ? e : 0][0] : lds_tr(tb0 + b2_lds(c, e), 1024), wb = e < 10 ? bw[e < 10 ? e : 0][1] : lds_tr(tb1 + b2_lds(c, e), 1024); \
    m0 = mfma(wa, __builtin_bit_cast(bf8, af[e]), m0); m1 = mfma(wb, __builtin_bit_cast(bf8, af[e]), m1); } while (0)
  P2STEP(0, 22); P2STEP(1, 21); P2STEP(2, 20); P2STEP(3, 19); P2STEP(4, 18); P2STEP(5, 17); P2STEP(6, 16); P2STEP(7, 15);
  P2STEP(8, 14); P2STEP(9, 13); P2STEP(10, 12); P2STEP(11, 11); P2STEP(12, 10); P2STEP(13, 9); P2STEP(14, 8); P2STEP(15, 7);
  P2STEP(16, 6); P2STEP(17, 5); P2STEP(18, 4); P2STEP(19, 3); P2STEP(20, 2); P2STEP(21, 1);
  ST(10);
  poll(l < 4 ? flag_off(pm, 2 * c.ks4 + (l >> 1), (l & 1) * 4 + pt, 0) : OOB, c.rws, c.tlo, c.thi);
  PIN(gv);
  ST(11);
  u4 of[8];
#pragma unroll
  for (int e = 0; e < 8; ++e) of[e] = ld16_sc1(l * 16, c.rws, abase + (86 + 8 * c.ks4 + e) * 1024);
  f16v n0 = {}, n1 = {};
#define P2O(e, n) do { asm volatile("s_waitcnt vmcnt(" #n ")" : "+v"(of[e])::"memory"); \
    n0 = mfma(lds_tr(tb0 + b2_lds(c, 22 + e), 1024), __builtin_bit_cast(bf8, of[e]), n0); n1 = mfma(lds_tr(tb1 + b2_lds(c, 22 + e), 1024), __builtin_bit_cast(bf8, of[e]), n1); } while (0)
  P2O(0, 7); P2O(1, 6); P2O(2, 5); P2O(3, 4); P2O(4, 3); P2O(5, 2); P2O(6, 1); P2O(7, 0);
  const float g = __uint_as_float(gv), og = 1.f - g;
  BAR();
  ST(12);
  { const u32 rb = c.ks4 * 17408 + (c.th * 32 + (l & 31)) * 272 + c.hh * 16;
#pragma unroll
    for (int k = 0; k < 4; ++k) {
      f4 v0, v1;
#pragma unroll
      for (int i = 0; i < 4; ++i) { v0[i] = og * m0[4 * k + i] + g * n0[4 * k + i]; v1[i] = og * m1[4 * k + i] + g * n1[4 * k + i]; }
      *(LAS f4*)(size_t)(rb + k * 32) = v0; *(LAS f4*)(size_t)(rb + 128 + k * 32) = v1;
    } }
  BAR();
  { const u32 t = threadIdx.x, tok = t >> 3, cg = t & 7, rb = tok * 272 + cg * 32;
    f4 v0 = {}, v1 = {};
#pragma unroll
    for (int k = 0; k < 4; ++k) { v0 += *(LAS f4*)(size_t)(rb + k * 17408); v1 += *(LAS f4*)(size_t)(rb + k * 17408 + 16); }
    const u4 o = {pk2(v0[0], v0[1]), pk2(v0[2], v0[3]), pk2(v1[0], v1[1]), pk2(v1[2], v1[3])};
    *(u4*)((char*)out + (size_t)((c.tg32 >> 1) * 64 + tok) * 1024 + c.nt * 128 + cg * 16) = o; }
  if (b == 0 && c.w == 7) {
#pragma unroll
    for (int k = 0; k < 4; ++k) { const u32 i = l + 64 * k; poll(flag_off(i >> 4, i & 15, 0, 0), c.rws, c.tlo, c.thi); }
    u2 t = {c.tlo, c.thi}; st8_sc1(t, l == 0 ? WS_EPOCH : OOB, c.rws, 0);
  }
}

extern "C" void kernel_launch(void* const* d_in, const int* in_sizes, int n_in, void* d_out, int out_size, void* d_ws, size_t ws_size, hipStream_t stream) {
  (void)in_sizes; (void)n_in; (void)out_size; (void)ws_size;
  (void)hipFuncSetAttribute((const void*)gm_fused, hipFuncAttributeMaxDynamicSharedMemorySize, LDS_BYTES);
  gm_fused<<<256, 512, LDS_BYTES, stream>>>(d_in[0], d_in[1], d_in[2], d_in[4], d_in[5], d_in[6], d_in[7], d_in[8], d_out, d_ws);
}
```

```cpp
#include <hip/hip_runtime.h>
#include <hip/hip_bf16.h>

typedef unsigned long long u64;
typedef unsigned int u32;
typedef short s16x4 __attribute__((ext_vector_type(4)));
typedef __bf16 bf8 __attribute__((ext_vector_type(8)));
typedef __bf16 bf2 __attribute__((ext_vector_type(2)));
typedef float f2 __attribute__((ext_vector_type(2)));
typedef float f4 __attribute__((ext_vector_type(4)));
typedef float f16v __attribute__((ext_vector_type(16)));
typedef u32 u2 __attribute__((ext_vector_type(2)));
typedef u32 u4 __attribute__((ext_vector_type(4)));
typedef __amdgpu_buffer_rsrc_t rsrc_t;
#define LAS __attribute__((address_space(3)))
#define DI __device__ __forceinline__

constexpr int S_ = 2048, D_ = 512, F_ = 1376;
constexpr u32 NSTEP = 118;
constexpr u32 WS_FLAG = 4096;
constexpr u32 WS_KVP = WS_FLAG + 16 * 16 * 4 * 4 * 64;
constexpr u32 WS_ZP = WS_KVP + 256 * 2048;
constexpr u32 WS_G = WS_ZP + 256 * 128;
constexpr u32 WS_A2 = WS_G + 2048 * 4;
constexpr u32 ASTR = (NSTEP + 1) * 1024;
constexpr u32 WS_END = WS_A2 + 64 * ASTR;
constexpr u32 WS_DBG = WS_END;
constexpr u32 LDS_BYTES = 163840;
constexpr u32 SLOT = 40960, CELL = 8192, MISC = 18 * CELL;
constexpr u32 KSCR = 3 * SLOT + 16384;
constexpr u32 OOB = 0x7ffffff0u;
constexpr u32 SPIN_CAP = 1u << 18;
#define ROTMASK 1
#define P2MAP 0
#define PFS 3
#define ROTX 1
#define PF1 0
#define DEEP 0
#define TYMASK 1
#define TAILBF 1
#define PFTAB 0x4040102bu
#define CODE_SPAN 0
#define CODE_SKIP 4096

DI rsrc_t mk_rsrc(const void* p, u32 bytes) { return __builtin_amdgcn_make_buffer_rsrc((void*)p, (short)0, (int)bytes, 0x00020000); }
template <u32 li> DI void dma(u32 lb, u32 voff, rsrc_t r, u32 soff) {
  asm volatile("s_add_u32 m0, %0, %1\n\ts_nop 0\n\tbuffer_load_dwordx4 %2, %3, %4 offen lds" ::"s"(lb), "i"(li), "v"(voff), "s"(r), "s"(soff) : "memory", "scc");
}
template <u32 li, u32 si> DI void dma2s(u32 lb, u32 voff, rsrc_t r, u32 sb) {
  u32 t;
  asm volatile("s_add_u32 m0, %1, %2\n\ts_add_u32 %0, %5, %6\n\tbuffer_load_dwordx4 %3, %4, %0 offen sc1 lds" : "=&s"(t) : "s"(lb), "i"(li), "v"(voff), "s"(r), "s"(sb), "i"(si) : "memory", "scc");
}
template <u32 li, u32 si> DI void dma2m(u64 mask, u32 lb, u32 voff, rsrc_t r, u32 sb) {
  u32 t;
  asm volatile("s_mov_b64 exec, %7\n\ts_add_u32 m0, %1, %2\n\ts_add_u32 %0, %5, %6\n\tbuffer_load_dwordx4 %3, %4, %0 offen lds\n\ts_mov_b64 exec, -1"
               : "=&s"(t) : "s"(lb), "i"(li), "v"(voff), "s"(r), "s"(sb), "i"(si), "s"(mask) : "memory", "scc");
}
template <u32 li, u32 si> DI void dma2(u32 lb, u32 voff, rsrc_t r, u32 sb) {
  u32 t;
  asm volatile("s_add_u32 m0, %1, %2\n\ts_add_u32 %0, %5, %6\n\tbuffer_load_dwordx4 %3, %4, %0 offen lds" : "=&s"(t) : "s"(lb), "i"(li), "v"(voff), "s"(r), "s"(sb), "i"(si) : "memory", "scc");
}
template <u32 imm = 0> DI u4 ld16_sc1(u32 voff, rsrc_t r, u32 soff) { u4 v; asm volatile("buffer_load_dwordx4 %0, %1, %2, %3 offen offset:%4 sc1" : "=v"(v) : "v"(voff), "s"(r), "s"(soff), "i"(imm) : "memory"); return v; }
DI u2 ld8_sc1(u32 voff, rsrc_t r, u32 soff) { u2 v; asm volatile("buffer_load_dwordx2 %0, %1, %2, %3 offen sc1" : "=v"(v) : "v"(voff), "s"(r), "s"(soff) : "memory"); return v; }
DI u32 ld4_sc1(u32 voff, rsrc_t r, u32 soff) { u32 v; asm volatile("buffer_load_dword %0, %1, %2, %3 offen sc1" : "=v"(v) : "v"(voff), "s"(r), "s"(soff) : "memory"); return v; }
DI u32 ld4(u32 voff, rsrc_t r, u32 soff) { u32 v; asm volatile("buffer_load_dword %0, %1, %2, %3 offen" : "=v"(v) : "v"(voff), "s"(r), "s"(soff) : "memory"); return v; }
DI u32 ld2(u32 voff, rsrc_t r, u32 soff) { u32 v; asm volatile("buffer_load_ushort %0, %1, %2, %3 offen" : "=v"(v) : "v"(voff), "s"(r), "s"(soff) : "memory"); return v; }
template <u32 imm = 0> DI void st16_sc1(u4 v, u32 voff, rsrc_t r, u32 soff) { asm volatile("buffer_store_dwordx4 %0, %1, %2, %3 offen offset:%4 sc1\n\ts_nop 1" ::"v"(v), "v"(voff), "s"(r), "s"(soff), "i"(imm) : "memory"); }
DI void st8_sc1(u2 v, u32 voff, rsrc_t r, u32 soff) { asm volatile("buffer_store_dwordx2 %0, %1, %2, %3 offen sc1\n\ts_nop 1" ::"v"(v), "v"(voff), "s"(r), "s"(soff) : "memory"); }
DI void st4_sc1(u32 v, u32 voff, rsrc_t r, u32 soff) { asm volatile("buffer_store_dword %0, %1, %2, %3 offen sc1\n\ts_nop 1" ::"v"(v), "v"(voff), "s"(r), "s"(soff) : "memory"); }
#define WAITV(n) asm volatile("s_waitcnt vmcnt(" #n ")" ::: "memory")
#define BARV(n) asm volatile("s_waitcnt vmcnt(" #n ") lgkmcnt(0)\n\ts_barrier" ::: "memory")
#define BAR() asm volatile("s_waitcnt lgkmcnt(0)\n\ts_barrier" ::: "memory")
#define PIN(x) asm volatile("" : "+v"(x))
#define SCHED() __builtin_amdgcn_sched_barrier(0)

DI bf8 lds_b128(u32 a) { return *(LAS bf8*)(size_t)a; }
DI f4 lds_f4(u32 a) { return *(LAS f4*)(size_t)a; }
DI void lds_st(u32 a, f4 v) { *(LAS f4*)(size_t)a = v; }
DI bf8 lds_tr(u32 a, u32 d) {
  s16x4 lo = __builtin_amdgcn_ds_read_tr16_b64_v4i16((LAS s16x4*)(size_t)a);
  s16x4 hi = __builtin_amdgcn_ds_read_tr16_b64_v4i16((LAS s16x4*)(size_t)(a + d));
  return __builtin_bit_cast(bf8, __builtin_shufflevector(lo, hi, 0, 1, 2, 3, 4, 5, 6, 7));
}
DI bf8 asbf(u4 v) { return __builtin_bit_cast(bf8, v); }
DI f16v mfma(bf8 a, bf8 b, f16v c) { return __builtin_amdgcn_mfma_f32_32x32x16_bf16(a, b, c, 0, 0, 0); }
DI u32 pk2(float a, float b) { f2 v = {a, b}; return __builtin_bit_cast(u32, __builtin_convertvector(v, bf2)); }
DI u4 pack8(const f16v& x, int s) { u4 r = {pk2(x[8 * s], x[8 * s + 1]), pk2(x[8 * s + 2], x[8 * s + 3]), pk2(x[8 * s + 4], x[8 * s + 5]), pk2(x[8 * s + 6], x[8 * s + 7])}; return r; }
DI float bflo(u32 v) { return __uint_as_float(v << 16); }
DI float bfhi(u32 v) { return __uint_as_float(v & 0xffff0000u); }
DI float sel(u32 m, float a, float b) { return __uint_as_float((m & __float_as_uint(a)) | (~m & __float_as_uint(b))); }
DI f2 phi2(float x, float y) {
  const f2 t = {x, y}, L = {1.4426950408889634f, 1.4426950408889634f}, eps = {1e-6f, 1e-6f};
  const f2 a = t * L;
  const f2 e = {__builtin_amdgcn_fmed3f(__builtin_amdgcn_exp2f(a.x), 0.f, 1.f), __builtin_amdgcn_fmed3f(__builtin_amdgcn_exp2f(a.y), 0.f, 1.f)};
  f2 m = t + eps; m.x = __builtin_fmaxf(m.x, 1e-6f); m.y = __builtin_fmaxf(m.y, 1e-6f);
  return m + e;
}
DI u32 gelu2(float x, float y) {
  const f2 v = {x, y}, c1 = {-0.1029432397f, -0.1029432397f}, c0 = {-2.3022081985f, -2.3022081985f}, one = {1.f, 1.f};
  const f2 w = v * __builtin_elementwise_fma(v * v, c1, c0);
  const f2 e = {__builtin_amdgcn_exp2f(w.x), __builtin_amdgcn_exp2f(w.y)};
  const f2 d = e + one;
  const f2 r = {__builtin_amdgcn_rcpf(d.x), __builtin_amdgcn_rcpf(d.y)};
  return __builtin_bit_cast(u32, __builtin_convertvector(v * r, bf2));
}
DI u32 flag_off(u32 mt, u32 ng, u32 wv, u32 kind) { return WS_FLAG + ((((mt * 16 + ng) * 4 + wv) * 4) + kind) * 64; }

#define ST(i)
#define STR(i)

struct Ctx {
  rsrc_t rws, rw2, rwo;
  u32 lane, j, mt, ng;
  u32 tlo, thi;
  u32 lb, vb, sw2[2], swo[2], sa[2], so[2], d20;
};
DI bool flags_ok(const Ctx& c, u2 v, u32 voff) { return __all((voff == OOB) || (v.x == c.tlo && v.y == c.thi)); }
DI void poll(const Ctx& c, u32 voff) {
#pragma nounroll
  for (u32 n = 0; n < SPIN_CAP; ++n) {
    u2 v = ld8_sc1(voff, c.rws, 0);
    asm volatile("s_waitcnt vmcnt(0)" : "+v"(v)::"memory");
    if (flags_ok(c, v, voff)) break;
    __builtin_amdgcn_s_sleep(1);
  }
}
DI void put_flag(const Ctx& c, u32 kind) { u2 t = {c.tlo, c.thi}; st8_sc1(t, c.lane == 0 ? flag_off(c.mt, c.ng, c.j, kind) : OOB, c.rws, 0); }

constexpr int NBW = 14;
constexpr int BCELL[30] = {0, 1, 2, 3, 4, 5, 6, 7, 8, 9, 10, 11, 12, 13, 14, 15, 16, 17, 13, 12, 11, 7, 9, 0, 18, 14, 15, 16, 17, 13};
constexpr int ACELL[30] = {0, 1, 2, 3, 4, 5, 6, 7, 8, 9, 10, 0, 1, 18, 19, 2, 3, 4, 5, 6, 8, 10, 1, 19, 2, 3, 4, 5, 6, 12};
constexpr bool is_o(int e) { return e >= 22; }
constexpr int ESTEP[30] = {0, 2, 8, 10, 1, 3, 9, 11, 4, 6, 12, 5, 7, 13, 14, 16, 18, 20, 15, 17, 19, 21, 0, 2, 4, 6, 1, 3, 5, 7};
template <int e> DI void ldb(const Ctx& c) {
  constexpr int st = ESTEP[e];
  const u32 sb = is_o(e) ? c.swo[st & 1] : c.sw2[st & 1];
  dma2<BCELL[e] * CELL, st * 16384>(c.lb, c.vb, is_o(e) ? c.rwo : c.rw2, sb); dma2<BCELL[e] * CELL + 1024, st * 16384 + 8192>(c.lb, c.vb, is_o(e) ? c.rwo : c.rw2, sb);
}
template <int a> DI void lda(const Ctx& c) {
  constexpr int st = ESTEP[a];
  const u32 sb = is_o(a) ? c.so[st & 1] : st < 20 ? c.sa[st & 1] : c.sa[st & 1] - c.d20;
  dma2s<ACELL[a] * CELL, st * 1024>(c.lb, c.lane * 16, c.rws, sb); dma2s<ACELL[a] * CELL + 1024, st * 1024 + ASTR>(c.lb, c.lane * 16, c.rws, sb);
}

struct Frag { bf8 x[2], w[3]; };
template <int MODE, int st> DI bf8 rd_w1(u32 aw, u32 a1) { if constexpr (MODE == 1) return lds_tr(aw + 8192 + st * 2048, 512); else return lds_tr(a1 + st * 4096, 1024); }
template <int MODE, int st> DI bf8 rd_w2(u32 aw, u32 a2) {
  if constexpr (MODE == 3) return lds_b128(a2 + (st >> 1) * 128 + (st & 1) * 32);
  else if constexpr (MODE == 2) return lds_tr(aw + 16384 + st * 2048, 512);
  else return lds_tr(a2 + st * 4096, 1024);
}
template <int MODE> DI f16v mm(bf8 w, bf8 x, f16v a) { if constexpr (MODE == 1) return mfma(x, w, a); else return mfma(w, x, a); }
template <int MODE, int st> DI void p1_step(const Frag& f, Frag& n, f16v (&a)[3][2], u32 ax, u32 aw, u32 a1, u32 a2) {
  if constexpr (MODE < 2) {
    a[0][0] = mm<MODE>(f.w[0], f.x[0], a[0][0]); SCHED();
    n.x[0] = lds_b128(ax); n.w[0] = lds_tr(aw + st * 2048, 512); SCHED();
    a[0][1] = mm<MODE>(f.w[0], f.x[1], a[0][1]); SCHED();
    n.x[1] = lds_b128(ax + 4096); n.w[1] = rd_w1<MODE, st>(aw, a1); SCHED();
    a[1][0] = mm<MODE>(f.w[1], f.x[0], a[1][0]); SCHED();
    if constexpr (MODE == 0) { n.w[2] = rd_w2<MODE, st>(aw, a2); SCHED(); }
    a[1][1] = mm<MODE>(f.w[1], f.x[1], a[1][1]);
    if constexpr (MODE == 0) { a[2][0] = mfma(f.w[2], f.x[0], a[2][0]); a[2][1] = mfma(f.w[2], f.x[1], a[2][1]); }
    SCHED();
  } else {
    n.x[0] = lds_b128(ax); n.w[2] = rd_w2<MODE, st>(aw, a2); n.x[1] = lds_b128(ax + 4096); SCHED();
    a[2][0] = mfma(f.w[2], f.x[0], a[2][0]); a[2][1] = mfma(f.w[2], f.x[1], a[2][1]); SCHED();
  }
}
template <int MODE, int st> DI void p1_read(Frag& f, u32 ax, u32 aw, u32 a1, u32 a2) {
  f.x[0] = lds_b128(ax); f.x[1] = lds_b128(ax + 4096);
  if constexpr (MODE < 2) { f.w[0] = lds_tr(aw + st * 2048, 512); f.w[1] = rd_w1<MODE, st>(aw, a1); }
  if constexpr (MODE != 1) f.w[2] = rd_w2<MODE, st>(aw, a2);
}
template <int MODE> DI void pass1(f16v (&a)[3][2], const u32 (&lx)[4], u32 lw, u32 l1, u32 l2) {
  Frag f[4];
  p1_read<MODE, 0>(f[0], lx[0], lw, l1, l2); p1_read<MODE, 1>(f[1], lx[1], lw, l1, l2);
  if constexpr (MODE < 2) a[0][0] = a[0][1] = a[1][0] = a[1][1] = f16v{};
  if constexpr (MODE != 1) a[2][0] = a[2][1] = f16v{};
  u32 base = 0;
  auto stage = [&]() {
    p1_step<MODE, 2>(f[0], f[2], a, lx[2] + base, lw + base, l1 + base, l2 + base);
    p1_step<MODE, 3>(f[1], f[3], a, lx[3] + base, lw + base, l1 + base, l2 + base);
    base = base == 3 * SLOT ? 0 : base + SLOT;
    BAR();
    p1_step<MODE, 0>(f[2], f[0], a, lx[0] + base, lw + base, l1 + base, l2 + base);
    p1_step<MODE, 1>(f[3], f[1], a, lx[1] + base, lw + base, l1 + base, l2 + base);
  };
  if constexpr (MODE < 2) stage();
#pragma nounroll
  for (u32 s = MODE < 2; s < 8; ++s) stage();
}
DI void gelu_store(const Ctx& c, f16v& g, u32 tg32, u32 chunk) {
  PIN(g);
  u4 lo, hi;
#pragma unroll
  for (int t = 0; t < 4; ++t) { lo[t] = gelu2(g[2 * t], g[2 * t + 1]); hi[t] = gelu2(g[8 + 2 * t], g[9 + 2 * t]); }
  const u32 so = WS_A2 + tg32 * ASTR + chunk * 2048;
  st16_sc1(lo, c.lane * 16, c.rws, so);
  st16_sc1<1024>(hi, c.lane * 16, c.rws, so);
}
DI f16v bias_acc(u32 hh, u32 bv, u32 h, f16v z) {
  const u4 A = {hh == h ? bv : 0u, 0u, 0u, 0u}, B = {0x3f80u, 0u, 0u, 0u};
  return mfma(asbf(A), asbf(B), z);
}

__global__ __launch_bounds__(512, 2) void gm_fused(const void* __restrict__ x, const void* __restrict__ wqkv, const void* __restrict__ w1,
                                                   const void* __restrict__ w2, const void* __restrict__ wo, void* __restrict__ ws,
                                                   const void* __restrict__ b1, void* __restrict__ out, const void* __restrict__ b2, const void* __restrict__ gw) {
  Ctx c;
  ST(0);
  u64 pc; asm volatile("s_getpc_b64 %0" : "=s"(pc)::"memory");
  const u32 b = blockIdx.x, xcd = b & 7, bi = b >> 3;
  const u32 w = __builtin_amdgcn_readfirstlane(threadIdx.x >> 6);
  c.lane = threadIdx.x & 63; c.j = w & 3;
  const u32 l = c.lane, jj = bi >> 3, odd = jj & 1, sl = xcd >> 1, hh = l >> 5;
  const bool kvt = jj < 2;
  const u32 g8 = 2 * sl + odd;
  c.mt = 8 * (xcd & 1) + (bi & 7); c.ng = kvt ? g8 : 8 + g8;
  const u32 s0 = (((bi & 7) + jj) & ROTMASK) ^ (ROTX ? (2 * sl) ^ (4 * (xcd & 1)) : 0);
  SCHED();
  const u32 hca = 11 * sl + 4 * odd, hcb = kvt ? 11 * sl + 7 + 2 * odd : hca + 2;
  const bool gwg = kvt && g8 == 7;
  const u32 nt = P2MAP ? 4 * (xcd & 1) + jj : bi >> 2, m64 = P2MAP ? 8 * sl + (bi & 7) : 4 * xcd + (bi & 3);
  const u32 pm = m64 >> 1, pt = m64 & 1, pi = g8 * 2;
  const u32 hsw = (((l >> 2) & 1) ^ ((l >> 4) & 1)) * 64 + (l & 3) * 16;
  asm volatile("" ::"s"(x), "s"(wqkv), "s"(w1), "s"(w2), "s"(wo), "s"(ws), "s"(b1), "s"(out), "s"(b2), "s"(gw));

  if (w < 4) {
    __builtin_amdgcn_s_setprio(2);
    const u32 tk = c.j & 1, cg = c.j >> 1, q = (l & 15) >> 2, tg = c.mt * 4 + 2 * tk;
    u32 pf1 = 0;
    if (PF1) {
      const u32 k6 = (l * 43) >> 8, i6 = b * 6 + l - 6 * k6;
      const u32 o = w == 0 ? (b * 8 + (l & 7)) * 1024 + (PF1 + (l >> 3)) * 128 : (PF1 + k6) * (w == 1 ? 196608 : 176128) + i6 * 128;
      const bool on = w == 0 ? (l >> 3) < 8 - PF1 : w < 3 && k6 < 8 - PF1 && (w == 1 || i6 < 1376);
      pf1 = ld4(on ? o : OOB, mk_rsrc(w == 0 ? x : w == 1 ? wqkv : w1, w == 0 ? S_ * D_ * 2 : w == 1 ? D_ * 1536 * 2 : D_ * F_ * 2), 0);
    }
    pf1 = ld4(l < 5 ? ((bi * 4 + w) * 5 + l) * 64 : OOB, mk_rsrc((const void*)pc, 40960), 0);
    c.rws = mk_rsrc(ws, WS_END);
    u32 bv = ld2((((kvt || hh) ? hcb : hca) + cg) * 64 + (l & 31) * 2, mk_rsrc(b1, F_ * 2), 0);
    u32 bvo = ld2(w == 0 ? (nt * 64 + l) * 2 : OOB, mk_rsrc(b2, D_ * 2), 0);
    u2 ep = ld8_sc1(0, c.rws, 0);
#define SMALL_LANDED() do { asm volatile("s_waitcnt vmcnt(0)" : "+v"(bv), "+v"(bvo), "+v"(ep), "+v"(pf1)::"memory"); \
      const u64 T_ = (((u64)(u32)__builtin_amdgcn_readfirstlane(ep.y) << 32) | (u32)__builtin_amdgcn_readfirstlane(ep.x)) * 6364136223846793005ULL + 1442695040888963407ULL; \
      c.tlo = (u32)T_; c.thi = (u32)(T_ >> 32); } while (0)
    u32 lx[4], lw;
    { const u32 r = l & 31, e = hh ^ ((r >> 1) & 7);
#pragma unroll
      for (int s = 0; s < 4; ++s) lx[s] = (tk * 64 + r) * 128 + (((2 * s) ^ e) * 16);
      lw = 16384 + (8 * hh + q) * 128 + ((cg ^ (q >> 1)) * 64) + ((l >> 4) & 1) * 32 + (l & 3) * 8; }
    asm volatile("s_barrier" : "+v"(lx[0]), "+v"(lx[1]), "+v"(lx[2]), "+v"(lx[3]), "+v"(lw)::"memory");
    ST(1);
    bf8 bw[NBW][2];
    u32 tb[3][2];
    u4 pq[4];
    const u32 hm = 0u - ((l >> 4) & 1);
#define RDW(ce, t) lds_tr(tb[(ce) / 7][t] + ((ce) % 7) * CELL, 1024)
#define BWALL() do { const u32 t_ = (4 * hh + q) * 128 + ((l >> 4) & 1) * 32 + (l & 3) * 8 + c.j * 2048 + ((q >> 1) * 64); \
      _Pragma("unroll") for (int k = 0; k < 3; ++k) { tb[k][0] = t_ + k * 7 * CELL; tb[k][1] = (t_ ^ 64) + k * 7 * CELL; } \
      asm volatile("" : "+v"(tb[0][0]), "+v"(tb[0][1]), "+v"(tb[1][0]), "+v"(tb[1][1]), "+v"(tb[2][0]), "+v"(tb[2][1])); \
      _Pragma("unroll") for (int e = 0; e < NBW; ++e) { bw[e][0] = RDW(BCELL[e], 0); bw[e][1] = RDW(BCELL[e], 1); } SCHED(); } while (0)
    u4 A0, A1;
    auto o_read = [&]() {
      const u4 kvp = *(LAS u4*)(size_t)(MISC + 8192 + cg * 1024 + l * 16);
      const u32 zo = MISC + 10240 + cg * 128 + (((l >> 4) & 1) ^ 1) * 32 + hh * 8;
      const u2 za = *(LAS u2*)(size_t)zo, zb = *(LAS u2*)(size_t)(zo + 16);
      const u4 zp = {za.x, za.y, zb.x, zb.y};
      A0 = (zp & hm) | (kvp & ~hm); A1 = (kvp & hm) | (zp & ~hm);
    };
    auto make_o = [&]() {
      const f16v zero = {};
#pragma unroll
      for (int t = 0; t < 2; ++t) {
        SCHED();
        const f16v o0 = mfma(asbf(A0), asbf(pq[2 * t]), zero), o1 = mfma(asbf(A1), asbf(pq[2 * t + 1]), zero);
        SCHED();
        const float r0 = __builtin_amdgcn_rcpf(o0[8] + 1e-6f), r1_ = __builtin_amdgcn_rcpf(o1[0] + 1e-6f);
        u4 lo, hi;
#pragma unroll
        for (int i = 0; i < 4; ++i) { lo[i] = pk2(o0[2 * i] * r0, o0[2 * i + 1] * r0); hi[i] = pk2(o1[8 + 2 * i] * r1_, o1[9 + 2 * i] * r1_); }
        const u32 so = WS_A2 + (tg + t) * ASTR + (86 + 4 * g8 + 2 * cg) * 1024;
        st16_sc1(lo, l * 16, c.rws, so); st16_sc1<1024>(hi, l * 16, c.rws, so);
      }
    };
    {
      f16v a[3][2];
      if (kvt) {
        const bool gate = gwg && cg == 1;
        pass1<1>(a, lx, lw, 0, 0);
        SMALL_LANDED();
        ST(2);
        f2 zz = {0.f, 0.f};
        f16v kv = {};
#pragma unroll
        for (int t = 0; t < 4; ++t) {
          u4 pk;
#pragma unroll
          for (int i = 0; i < 4; ++i) { const f2 p = phi2(a[0][t >> 1][8 * (t & 1) + 2 * i], a[0][t >> 1][8 * (t & 1) + 2 * i + 1]); zz += p; pk[i] = pk2(p.x, p.y); }
          kv = mfma(asbf(pk), asbf(pack8(a[1][t >> 1], t & 1)), kv);
        }
        float zs = zz.x + zz.y;
        { const auto r = __builtin_amdgcn_permlane32_swap(__float_as_uint(zs), __float_as_uint(zs), false, false); zs = __uint_as_float(r[0]) + __uint_as_float(r[1]); }
        u32 hm2 = hm; PIN(hm2);
        f4 s0v, s1v;
#pragma unroll
        for (int i = 0; i < 4; ++i) { s0v[i] = sel(hm2, kv[8 + i], kv[i]); s1v[i] = sel(hm2, kv[12 + i], kv[4 + i]); }
        lds_st(KSCR + c.j * 1024 + l * 16, s0v); lds_st(KSCR + 4096 + c.j * 1024 + l * 16, s1v);
        *(LAS float*)(size_t)(KSCR + 8192 + c.j * 128 + (l & 31) * 4) = zs;
        BAR();
        { const u32 ra = KSCR + tk * 4096 + cg * 2048 + l * 16, rz = KSCR + 8192 + cg * 256 + (l & 7) * 16;
          const f4 r0 = lds_f4(ra), r1_ = lds_f4(ra + 1024), za = lds_f4(rz), zb = lds_f4(rz + 128);
          const f4 s = r0 + r1_, z = za + zb;
          const u32 o = (pi + cg) * 16 + c.mt;
          st16_sc1((u4){pk2(s[0], s[1]), c.tlo, pk2(s[2], s[3]), c.thi}, l * 16, c.rws, WS_KVP + o * 2048 + tk * 1024);
          st16_sc1((u4){pk2(z[0], z[1]), c.tlo, pk2(z[2], z[3]), c.thi}, (l < 8 && tk) ? l * 16 : OOB, c.rws, WS_ZP + o * 128); }
        ST(3);
        if (gate) pass1<3>(a, lx, lw, 0, 32768 + 64 + hh * 16); else pass1<2>(a, lx, lw, 0, 0);
        ST(10);
        if (gate) {
          const float g0 = __builtin_amdgcn_rcpf(1.f + __builtin_amdgcn_exp2f(-1.4426950408889634f * (hh ? a[2][1][0] : a[2][0][0])));
          st4_sc1(__float_as_uint(g0), l * 4, c.rws, WS_G + tg * 128);
        } else {
          a[2][0] = bias_acc(hh, bv, 0, a[2][0]); a[2][1] = bias_acc(hh, bv, 0, a[2][1]);
          gelu_store(c, a[2][0], tg, hcb + cg); gelu_store(c, a[2][1], tg + 1, hcb + cg);
        }
        ST(4);
        WAITV(0); put_flag(c, 0);
        BAR();
        BWALL();
        BAR();
        ST(15);
      } else {
        { const u32 l1 = 24576 + (8 * hh + q) * 256 + ((cg ^ q) * 64) + ((l >> 4) & 1) * 32 + (l & 3) * 8; pass1<0>(a, lx, lw, l1, l1 ^ 128); }
        SMALL_LANDED();
        ST(2);
        a[1][0] = bias_acc(hh, bv, 0, a[1][0]); a[1][1] = bias_acc(hh, bv, 0, a[1][1]);
        a[2][0] = bias_acc(hh, bv, 1, a[2][0]); a[2][1] = bias_acc(hh, bv, 1, a[2][1]);
        auto phiq = [&]() {
#pragma unroll
          for (int t = 0; t < 4; ++t) {
#pragma unroll
            for (int i = 0; i < 4; ++i) { const f2 p = phi2(a[0][t >> 1][8 * (t & 1) + 2 * i], a[0][t >> 1][8 * (t & 1) + 2 * i + 1]); pq[t][i] = pk2(p.x, p.y); }
            PIN(pq[t]);
          } };
        gelu_store(c, a[1][0], tg, hca + cg); gelu_store(c, a[1][1], tg + 1, hca + cg);
        ST(3);
        BAR();
        BWALL();
        BAR();
        const bool full = !(odd && cg == 1);
        if (full) { gelu_store(c, a[2][0], tg, hcb + cg); WAITV(2); } else WAITV(0);
        put_flag(c, 1);
        if (full) gelu_store(c, a[2][1], tg + 1, hcb + cg);
        ST(4);
        phiq();
        WAITV(0); put_flag(c, 2);
        ST(10);
        ST(15);
      }
    }
    f16v m[2][2], n[2][2] = {};
    { const f16v z = {}; m[0][0] = bias_acc(hh, bvo, 0, z); m[0][1] = bias_acc(hh, bvo, 1, z); m[1][0] = m[0][0]; m[1][1] = m[0][1]; }
    u32 aa[3] = {c.j * 2048 + l * 16, c.j * 2048 + l * 16 + 7 * CELL, c.j * 2048 + l * 16 + 14 * CELL}, gv0, gv1;
    asm volatile("" : "+v"(aa[0]), "+v"(aa[1]), "+v"(aa[2]));
    bf8 xa[3][2], wn[3][2];
#define RDA(a, t) do { constexpr int ce = ACELL[(a) < 30 ? (a) : 29]; xa[(a) % 3][t] = lds_b128(aa[ce / 7] + (ce % 7) * CELL + (t) * 1024); } while (0)
#define RDB(e, t) do { if ((e) >= NBW) { constexpr int ce = BCELL[(e) < 30 ? (e) : 29]; wn[(e) % 3][t] = RDW(ce, t); } } while (0)
#define RD01() do { RDA(0, 0); RDA(0, 1); RDA(1, 0); RDA(1, 1); SCHED(); } while (0)
#define STEP(a, X) do { const bf8 w0_ = (a) < NBW ? bw[(a) < NBW ? (a) : 0][0] : wn[(a) % 3][0], w1_ = (a) < NBW ? bw[(a) < NBW ? (a) : 0][1] : wn[(a) % 3][1]; \
      X[0][0] = mfma(w0_, xa[(a) % 3][0], X[0][0]); SCHED(); if ((a) < 28) { RDA((a) + 2, 0); RDB((a) + 2, 0); } SCHED(); \
      X[1][0] = mfma(w0_, xa[(a) % 3][1], X[1][0]); SCHED(); if ((a) < 28) { RDA((a) + 2, 1); RDB((a) + 2, 1); } SCHED(); \
      X[0][1] = mfma(w1_, xa[(a) % 3][0], X[0][1]); X[1][1] = mfma(w1_, xa[(a) % 3][1], X[1][1]); SCHED(); } while (0)
#define MAKE_O() do { if (!kvt) { o_read(); make_o(); } ST(16); } while (0)
#define O_FLAG() do { WAITV(0); const u2 t_ = {c.tlo, c.thi}; st8_sc1(t_, (l == 0 && !kvt) ? flag_off(c.mt, c.ng, c.j, 0) : OOB, c.rws, 0); } while (0)
#define GATE_LOADS() do { gv0 = ld4_sc1((l & 31) * 4, c.rws, WS_G + m64 * 256); gv1 = ld4_sc1((l & 31) * 4, c.rws, WS_G + m64 * 256 + 128); } while (0)
    BAR();
    ST(9);
    MAKE_O();
    RD01();
    asm volatile("s_waitcnt lgkmcnt(0)\n\ts_barrier" ::: "memory");
    STEP(0, m);
    STEP(1, m);
    asm volatile("s_waitcnt lgkmcnt(0)\n\ts_barrier" ::: "memory");
    STEP(2, m);
    STEP(3, m);
    asm volatile("s_waitcnt lgkmcnt(0)\n\ts_barrier" ::: "memory");
    O_FLAG();
    STEP(4, m);
    STEP(5, m);
    asm volatile("s_waitcnt lgkmcnt(0)\n\ts_barrier" ::: "memory");
    STEP(6, m);
    STEP(7, m);
    asm volatile("s_waitcnt lgkmcnt(0)\n\ts_barrier" ::: "memory");
    STEP(8, m);
    STEP(9, m);
    asm volatile("s_waitcnt lgkmcnt(0)\n\ts_barrier" ::: "memory");
    STEP(10, m);
    STEP(11, m);
    asm volatile("s_waitcnt lgkmcnt(0)\n\ts_barrier" ::: "memory");
    STEP(12, m);
    STEP(13, m);
    asm volatile("s_waitcnt lgkmcnt(0)\n\ts_barrier" ::: "memory");
    GATE_LOADS();
    STEP(14, m);
    STEP(15, m);
    asm volatile("s_waitcnt lgkmcnt(0)\n\ts_barrier" ::: "memory");
    STEP(16, m);
    STEP(17, m);
    asm volatile("s_waitcnt lgkmcnt(0)\n\ts_barrier" ::: "memory");
    STEP(18, m);
    STEP(19, m);
    asm volatile("s_waitcnt lgkmcnt(0)\n\ts_barrier" ::: "memory");
    STEP(20, m);
    STEP(21, m);
    asm volatile("s_waitcnt lgkmcnt(0)\n\ts_barrier" ::: "memory");
    STEP(22, n);
    STEP(23, n);
    asm volatile("s_waitcnt lgkmcnt(0)\n\ts_barrier" ::: "memory");
    STEP(24, n);
    STEP(25, n);
    asm volatile("s_waitcnt lgkmcnt(0)\n\ts_barrier" ::: "memory");
    STEP(26, n);
    STEP(27, n);
    STEP(28, n);
    STEP(29, n);
    asm volatile("s_waitcnt vmcnt(0)\n\ts_barrier" : "+v"(gv0), "+v"(gv1), "+v"(n[0][0]), "+v"(n[0][1]), "+v"(n[1][0]), "+v"(n[1][1])::"memory");
    ST(12);
    __builtin_amdgcn_s_setprio(0);
#pragma unroll
    for (int t = 0; t < 2; ++t) {
      const float g = __uint_as_float(t ? gv1 : gv0), og = 1.f - g;
      const u32 rb = c.j * 9216 + (t * 32 + (l & 31)) * 144 + hh * 16;
#pragma unroll
      for (int k = 0; k < 4; ++k) {
        u32 p[4];
#pragma unroll
        for (int i = 0; i < 4; ++i) { const int r = 8 * (k & 1) + 2 * i; p[i] = pk2(og * m[t][k >> 1][r] + g * n[t][k >> 1][r], og * m[t][k >> 1][r + 1] + g * n[t][k >> 1][r + 1]); }
        const auto s0_ = __builtin_amdgcn_permlane32_swap(p[0], p[2], false, false), s1_ = __builtin_amdgcn_permlane32_swap(p[1], p[3], false, false);
        *(LAS u4*)(size_t)(rb + k * 32) = (u4){s0_[0], s1_[0], s0_[1], s1_[1]};
      }
    }
  } else {
    const rsrc_t rx = mk_rsrc(x, S_ * D_ * 2);
    const u32 vx0 = (c.mt * 128 + 32 * c.j + (l >> 3)) * 1024 + (((l & 7) ^ (l >> 4)) * 16), vx1 = (vx0 + 8192) ^ 64;
    asm volatile("s_nop 4" ::: "memory");
    dma<0>(c.j * 4096, vx0, rx, s0 * 128); dma<1024>(c.j * 4096, vx1, rx, s0 * 128);
    dma2<2048, 16384>(c.j * 4096, vx0, rx, s0 * 128); dma2<3072, 16384>(c.j * 4096, vx1, rx, s0 * 128);
    SCHED();
    const rsrc_t rq = mk_rsrc(wqkv, D_ * 1536 * 2), r1 = mk_rsrc(w1, D_ * F_ * 2);
    const rsrc_t rs1 = mk_rsrc(kvt ? wqkv : w1, kvt ? D_ * 1536 * 2 : D_ * F_ * 2);
    const u32 wrow = 16 * c.j + (l >> 3), qd = ((l >> 2) & 3) ^ (l >> 4);
    const u32 vw0 = wrow * 3072 + 128 * g8 + hsw + (kvt ? 1024 : 0);
    const u32 vw1 = kvt ? vw0 + 1024 : (16 * c.j + (l >> 4)) * 2752 + hca * 64 + qd * 64 + (l & 3) * 16;
    const u32 vw2 = kvt ? wrow * 2752 + hcb * 64 + hsw : vw1 + 8 * 2752;
    const u32 st1 = kvt ? 3072 : 2752, d1 = kvt ? 8 * 3072 : 4 * 2752, d2 = kvt ? 8 * 2752 : 4 * 2752;
    const u32 o1 = kvt ? 24576 + c.j * 2048 : 24576 + c.j * 4096, o2 = kvt ? o1 + 8192 : o1 + 2048;
    const u64 m2 = __builtin_amdgcn_ballot_w64(kvt ? !(gwg && (hsw & 64)) : !(odd && qd == 3)), m1 = kvt ? ~0ULL : m2;
    const rsrc_t rg = mk_rsrc(gw, D_ * 2);
    auto half0 = [&](u32 T, u32 base, bool p01, bool p2, bool px = true) {
      const u32 kb = s0 ^ T, lx = base + c.j * 4096;
      if (p2 && gwg && c.j == 0) dma2m<32768, 0>(0xF0F0ULL, base, ((l & 3) + (l >> 3) * 4) * 16, rg, kb * 128);
      if (px) dma<0>(lx, vx0, rx, kb * 128);
      if (p01) dma<16384>(base + c.j * 2048, vw0, rq, kb * (64 * 3072));
      if (px) dma<1024>(lx, vx1, rx, kb * 128);
      if (p01) dma2m<0, 0>(m1, base + o1, vw1, rs1, kb * 64 * st1);
      if (p2) dma2m<0, 0>(m2, base + o2, vw2, r1, kb * (64 * 2752));
    };
    auto half1 = [&](u32 T, u32 base, bool p01, bool p2, bool px = true) {
      const u32 kb = s0 ^ T, lx = base + c.j * 4096;
      if (px) dma2<2048, 16384>(lx, vx0, rx, kb * 128);
      if (p01) dma2<16384 + 1024, 8 * 3072>(base + c.j * 2048, vw0, rq, kb * (64 * 3072));
      if (px) dma2<3072, 16384>(lx, vx1, rx, kb * 128);
      if (p01) dma2m<1024, 0>(m1, base + o1, vw1, rs1, kb * 64 * st1 + d1);
      if (p2) dma2m<1024, 0>(m2, base + o2, vw2, r1, kb * (64 * 2752) + d2);
    };
    half0(0, 0, true, !kvt, false); half1(0, 0, true, !kvt, false);
    SCHED();
#define LBARV(n) asm volatile("s_waitcnt vmcnt(" #n ")\n\ts_barrier" ::: "memory")
#define LBAR() asm volatile("s_barrier" ::: "memory")
#define LBAR2(nk, nq) do { if (kvt) WAITV(nk); LBARV(nq); } while (0)
    u64 E, pfa;
    asm volatile("s_load_dwordx2 %0, %2, 0x0\n\ts_load_dwordx2 %1, %3, %4" : "=&s"(E), "=&s"(pfa) : "s"(ws), "s"(__builtin_amdgcn_kernarg_segment_ptr()), "s"(8 * (3 + c.j + (c.j == 3))) : "memory");
    u32 cw;
    { u32 o = (bi * 4 + c.j) * 256 + CODE_SKIP; o = o < CODE_SPAN ? o : 0;
      asm volatile("s_load_dword %0, %1, %2\n\ts_load_dword %0, %1, %3" : "=&s"(cw) : "s"(pc), "s"(o), "s"(o + 128) : "memory"); }
    half0(1, SLOT, true, !kvt); half1(1, SLOT, true, !kvt);
    LBAR2(8, 10);
    ST(1);
    half0(2, 2 * SLOT, true, !kvt); half1(2, 2 * SLOT, true, !kvt);
    LBAR2(8, 10);
    half0(3, 3 * SLOT, true, !kvt); half1(3, 3 * SLOT, true, !kvt);
    LBAR2(8, 10);
    half0(4, 0, true, !kvt); half1(4, 0, true, !kvt); half0(5, SLOT, true, !kvt);
    LBAR2(12, 15);
    half1(5, SLOT, true, !kvt); half0(6, 2 * SLOT, true, !kvt);
    asm volatile("s_waitcnt lgkmcnt(0)" : "+s"(E), "+s"(pfa), "+s"(cw)::"memory");
    c.rws = mk_rsrc(ws, WS_END); c.rw2 = mk_rsrc(w2, F_ * D_ * 2); c.rwo = mk_rsrc(wo, D_ * D_ * 2);
    c.lb = c.j * 2048; c.vb = (l >> 3) * 1024 + nt * 128 + hsw; c.d20 = c.j == 3 ? 2048 : 0;
    { const u32 ab = WS_A2 + 2 * m64 * ASTR, ty = (nt + m64) & TYMASK;
#pragma unroll
      for (int k = 0; k < 2; ++k) {
        const u32 d = k ? 0u - ty : ty;
        c.sw2[k] = (c.j * 22 + d) * 16384; c.swo[k] = (c.j * 8 + d) * 16384; c.sa[k] = ab + (c.j * 22 + d) * 1024; c.so[k] = ab + (86 + 8 * c.j + d) * 1024;
      } }
    { const u64 T = E * 6364136223846793005ULL + 1442695040888963407ULL; c.tlo = (u32)T; c.thi = (u32)(T >> 32); }
    const u32 pn = (u32)(PFTAB >> (8 * c.j)) & 0xff;
    rsrc_t rpf = mk_rsrc((const void*)pfa, c.j == 2 ? WS_END : pn << 15);
    u32 pfo = l < pn ? l * (c.j < 2 ? 128 : c.j == 2 ? ASTR : 32768) : OOB, pfs = c.j < 2 ? b * pn * 128 : c.j == 2 ? WS_A2 + NSTEP * 1024 : 0, pfv;
    asm volatile("" : "+s"(c.tlo), "+s"(c.thi), "+s"(rpf), "+s"(pfs), "+v"(pfo), "+v"(c.vb));
    LBAR2(12, 15);
    half1(6, 2 * SLOT, true, !kvt); half0(7, 3 * SLOT, true, !kvt);
    if (kvt) {
      LBARV(12); half1(7, 3 * SLOT, true, false); pfv = ld4(pfo, rpf, pfs); half0(0, 0, false, true);
      LBARV(12); half1(0, 0, false, true); half0(1, SLOT, false, true);
      LBARV(10); half1(1, SLOT, false, true); half0(2, 2 * SLOT, false, true); half1(2, 2 * SLOT, false, true);
      LBARV(12); half0(3, 3 * SLOT, false, true); half1(3, 3 * SLOT, false, true);
      LBAR();
#pragma nounroll
      for (u32 s = 0; s < 4; ++s) { LBARV(12); half0(s + 4, s * SLOT, false, true); half1(s + 4, s * SLOT, false, true); }
      LBARV(12); ldb<0>(c); ldb<1>(c);
      LBARV(10); ldb<2>(c); ldb<3>(c);
      LBARV(8); ldb<4>(c); ldb<5>(c);
      LBAR();
      ST(2);
      ldb<6>(c); ldb<7>(c); ldb<8>(c); ldb<9>(c); ldb<10>(c);
    } else {
      LBARV(15); half1(7, 3 * SLOT, true, true); pfv = ld4(pfo, rpf, pfs); ldb<0>(c); ldb<1>(c);
      LBARV(15); ldb<2>(c); ldb<3>(c); ldb<4>(c); ldb<5>(c);
      LBARV(13); ldb<6>(c); ldb<7>(c); ldb<8>(c); ldb<9>(c); ldb<10>(c);
      LBAR();
      ST(2);
    }
    static_assert(NBW == 14, "");
    ldb<11>(c); ldb<12>(c); ldb<13>(c); ldb<14>(c); ldb<15>(c);
    asm volatile("s_waitcnt vmcnt(4)\n\ts_barrier" : "+v"(pfv)::"memory");
#define FLAGS4(ng0, kind) (l < 4 ? flag_off(pm, (ng0) + 2 * c.j + (l >> 1), (l & 1) * 2 + pt, kind) : OOB)
    const u32 fearly = FLAGS4(8, 1), fo = FLAGS4(8, 0), fq = FLAGS4(8, 2), fk = l == 4 ? flag_off(pm, 7, 2 + pt, 0) : FLAGS4(0, 0);
    if (!kvt) {
      const u32 gi = (pi + (c.j >> 1)) * 16, gb = WS_KVP + gi * 2048 + (c.j & 1) * 1024, gz = WS_ZP + gi * 128;
      u4 p[16], z0, z1;
#define GLOADS() do { z0 = ld16_sc1(l * 16, c.rws, gz); z1 = ld16_sc1<1024>(l * 16, c.rws, gz); \
        _Pragma("unroll") for (int m = 0; m < 16; ++m) p[m] = (m & 1) ? ld16_sc1<2048>(l * 16, c.rws, gb + (m >> 1) * 4096) : ld16_sc1(l * 16, c.rws, gb + (m >> 1) * 4096); } while (0)
#define GWAIT(n) asm volatile("s_waitcnt vmcnt(" #n ")" : "+v"(p[0]), "+v"(p[1]), "+v"(p[2]), "+v"(p[3]), "+v"(p[4]), "+v"(p[5]), "+v"(p[6]), "+v"(p[7]), \
                     "+v"(p[8]), "+v"(p[9]), "+v"(p[10]), "+v"(p[11]), "+v"(p[12]), "+v"(p[13]), "+v"(p[14]), "+v"(p[15]), "+v"(z0), "+v"(z1)::"memory")
      z0 = ld16_sc1(l * 16, c.rws, gz); z1 = ld16_sc1<1024>(l * 16, c.rws, gz);
#pragma unroll
      for (int m = 0; m < 4; ++m) p[m] = (m & 1) ? ld16_sc1<2048>(l * 16, c.rws, gb + (m >> 1) * 4096) : ld16_sc1(l * 16, c.rws, gb + (m >> 1) * 4096);
      BAR();
#pragma unroll
      for (int m = 4; m < 16; ++m) p[m] = (m & 1) ? ld16_sc1<2048>(l * 16, c.rws, gb + (m >> 1) * 4096) : ld16_sc1(l * 16, c.rws, gb + (m >> 1) * 4096);
      ST(4);
      ldb<16>(c); ldb<17>(c);
      u2 pv = ld8_sc1(fearly, c.rws, 0);
      ldb<18>(c);
      GWAIT(2);
      PIN(pv);
      if (!flags_ok(c, pv, fearly)) poll(c, fearly);
      ST(8);
      lda<0>(c); lda<1>(c); lda<2>(c); lda<3>(c);
      __builtin_amdgcn_s_setprio(3);
#pragma nounroll
      for (u32 n = 0; n < SPIN_CAP; ++n) {
        u32 lo = (z0.y ^ c.tlo) | (z1.y ^ c.tlo), hi = (z0.w ^ c.thi) | (z1.w ^ c.thi);
#pragma unroll
        for (int m = 0; m < 16; ++m) { lo |= p[m].y ^ c.tlo; hi |= p[m].w ^ c.thi; }
        PIN(lo);
        if (__all((lo | hi) == 0)) break;
        ST(6);
#pragma nounroll
        for (; n < SPIN_CAP; ++n) {
          u4 y0 = ld16_sc1(l * 16, c.rws, gz), y1 = ld16_sc1<1024>(l * 16, c.rws, gz);
          asm volatile("s_waitcnt vmcnt(0)" : "+v"(y0), "+v"(y1)::"memory");
          if (__all(((y0.y ^ c.tlo) | (y0.w ^ c.thi) | (y1.y ^ c.tlo) | (y1.w ^ c.thi)) == 0)) break;
          __builtin_amdgcn_s_sleep(2);
        }
        GLOADS();
        GWAIT(0);
      }
      ST(5);
#define UNP(v) (f4){bflo(v.x), bfhi(v.x), bflo(v.z), bfhi(v.z)}
      f4 s = UNP(p[0]), kz = UNP(z0) + UNP(z1);
#pragma unroll
      for (int m = 1; m < 16; ++m) s += UNP(p[m]);
#pragma unroll
      for (int i = 0; i < 4; ++i) {
        float z = kz[i];
        z += __uint_as_float(__builtin_amdgcn_update_dpp(0, __float_as_uint(z), 0x128, 0xf, 0xf, false));
        const auto r = __builtin_amdgcn_permlane16_swap(__float_as_uint(z), __float_as_uint(z), false, false);
        z = __uint_as_float(r[0]) + __uint_as_float(r[1]);
        const auto t = __builtin_amdgcn_permlane32_swap(__float_as_uint(z), __float_as_uint(z), false, false);
        kz[i] = __uint_as_float(t[0]) + __uint_as_float(t[1]);
      }
      *(LAS u2*)(size_t)(MISC + 8192 + (c.j >> 1) * 1024 + l * 16 + (c.j & 1) * 8) = (u2){pk2(s[0], s[1]), pk2(s[2], s[3])};
      *(LAS u2*)(size_t)(MISC + 10240 + c.j * 64 + (l & 7) * 8) = (u2){pk2(kz[0], kz[1]), pk2(kz[2], kz[3])};
      __builtin_amdgcn_s_setprio(0);
      ST(16);
    } else {
      ldb<16>(c); ldb<17>(c);
      BAR();
      ST(4);
      u2 pv = ld8_sc1(fearly, c.rws, 0);
      ldb<18>(c);
      asm volatile("s_waitcnt vmcnt(2)" : "+v"(pv)::"memory");
      if (!flags_ok(c, pv, fearly)) poll(c, fearly);
      ST(8);
      lda<0>(c); lda<1>(c); lda<2>(c); lda<3>(c);
    }
    u2 ef[4];
    const bool closer = b == 0 && w == 7;
#define EPOCH_LOADS() do { if (closer) { _Pragma("unroll") for (int k = 0; k < 4; ++k) { const u32 i = l + 64 * k; ef[k] = ld8_sc1(flag_off(i >> 4, i & 15, 0, (i >> 3) & 1), c.rws, 0); } } } while (0)
    ldb<19>(c);
    ldb<20>(c);
    BARV(8);
    u2 pq = ld8_sc1(fq, c.rws, 0);
    lda<4>(c);
    lda<5>(c);
    lda<6>(c);
    BARV(11);
    lda<7>(c);
    asm volatile("s_waitcnt vmcnt(8)" : "+v"(pq)::"memory");
    if (!flags_ok(c, pq, fq)) poll(c, fq);
    ST(17);
    lda<8>(c);
    lda<9>(c);
    BARV(8);
    u2 pk = ld8_sc1(fk, c.rws, 0);
    lda<10>(c);
    lda<11>(c);
    lda<12>(c);
    BARV(11);
    lda<13>(c);
    asm volatile("s_waitcnt vmcnt(8)" : "+v"(pk)::"memory");
    if (!flags_ok(c, pk, fk)) poll(c, fk);
    ST(7);
    lda<14>(c);
    lda<15>(c);
    BARV(13);
    lda<16>(c);
    lda<17>(c);
    lda<18>(c);
    BARV(14);
    lda<19>(c);
    ldb<21>(c);
    lda<20>(c);
    BARV(16);
    u2 po = ld8_sc1(fo, c.rws, 0);
    ldb<22>(c);
    lda<21>(c);
    ldb<23>(c);
    BARV(19);
    asm volatile("s_waitcnt vmcnt(6)" : "+v"(po)::"memory");
    if (!flags_ok(c, po, fo)) poll(c, fo);
    ST(11);
    lda<22>(c);
    ldb<24>(c);
    BARV(19);
    lda<23>(c);
    ldb<25>(c);
    lda<24>(c);
    BARV(21);
    ldb<26>(c);
    lda<25>(c);
    ldb<27>(c);
    BARV(18);
    lda<26>(c);
    ldb<28>(c);
    lda<27>(c);
    BARV(16);
    ldb<29>(c);
    lda<28>(c);
    lda<29>(c);
    BARV(14);
    EPOCH_LOADS();
    BARV(6);
    BARV(0);
    if (closer) {
      asm volatile("" : "+v"(ef[0]), "+v"(ef[1]), "+v"(ef[2]), "+v"(ef[3]));
      bool ok = true;
#pragma unroll
      for (int k = 0; k < 4; ++k) ok = ok && ef[k].x == c.tlo && ef[k].y == c.thi;
      if (!__all(ok)) {
#pragma nounroll
        for (int k = 0; k < 4; ++k) { const u32 i = l + 64 * k; poll(c, flag_off(i >> 4, i & 15, 0, (i >> 3) & 1)); }
      }
      u2 t = {c.tlo, c.thi}; st8_sc1(t, l == 0 ? 0u : OOB, c.rws, 0);
    }
    BAR();
  }
  BAR();
  { const u32 t = threadIdx.x, tok = t >> 3, cgp = t & 7;
    u4 r[4];
#pragma unroll
    for (int k = 0; k < 4; ++k) r[k] = *(LAS u4*)(size_t)(tok * 144 + cgp * 16 + k * 9216);
    SCHED();
#define LO4(v) (f4){bflo(v.x), bfhi(v.x), bflo(v.y), bfhi(v.y)}
#define HI4(v) (f4){bflo(v.z), bfhi(v.z), bflo(v.w), bfhi(v.w)}
    const f4 v0 = (LO4(r[0]) + LO4(r[1])) + (LO4(r[2]) + LO4(r[3])), v1 = (HI4(r[0]) + HI4(r[1])) + (HI4(r[2]) + HI4(r[3]));
    const u4 o = {pk2(v0[0], v0[1]), pk2(v0[2], v0[3]), pk2(v1[0], v1[1]), pk2(v1[2], v1[3])};
    st16_sc1(o, tok * 1024 + cgp * 16 + m64 * 65536 + nt * 128, mk_rsrc(out, S_ * D_ * 2), 0); }
}

extern "C" void kernel_launch(void* const* d_in, const int* in_sizes, int n_in, void* d_out, int out_size, void* d_ws, size_t ws_size, hipStream_t stream) {
  (void)in_sizes; (void)n_in; (void)out_size; (void)ws_size;
  (void)hipFuncSetAttribute((const void*)gm_fused, hipFuncAttributeMaxDynamicSharedMemorySize, LDS_BYTES);
  gm_fused<<<256, 512, LDS_BYTES, stream>>>(d_in[0], d_in[1], d_in[4], d_in[6], d_in[2], d_ws, d_in[5], d_out, d_in[7], d_in[8]);
}
```
